```python
import torch

def _softmax(x, axis=-1):
    return torch.softmax(x, dim=axis)
def _gelu(x):
    return torch.nn.functional.gelu(x, approximate="tanh")
def _silu(x):
    return torch.nn.functional.silu(x)
def _sigmoid(x):
    return torch.sigmoid(x)
def _layernorm(x, axis=-1, eps=1e-5):
    m = x.mean(dim=axis, keepdim=True)
    v = x.var(dim=axis, keepdim=True, unbiased=False)
    return (x - m) / torch.sqrt(v + eps)
def _scatter_add(tgt, idx, src, axis=0):
    return tgt.clone().index_add_(axis, idx, src)
def _segment_sum(src, seg, n):
    out = torch.zeros((n,) + src.shape[1:], dtype=src.dtype)
    return out.index_add_(0, seg, src)
def _pad(x, pw):
    flat = tuple(v for pair in reversed(pw) for v in pair)
    return torch.nn.functional.pad(x, flat)

kernel_name = 'standalone__softmax_full_bidir__decode'

def setup_inputs():

    _w = (lambda a: (lambda t: t.to(torch.bfloat16) if t.is_floating_point() else t)(torch.as_tensor(a)))
    B, H, Sq, Skv, Dh = 2, 32, 1, 16384, 128
    return {
        "q": _w((lambda *s: torch.randn(*s, dtype=torch.bfloat16) * 0.8)(B, H, Sq, Dh)),
        "k": _w((lambda *s: torch.randn(*s, dtype=torch.bfloat16) * 0.8)(B, H, Skv, Dh)),
        "v": _w((lambda *s: torch.randn(*s, dtype=torch.bfloat16) * 0.05)(B, H, Skv, Dh)),
    }

def reference(q, k, v):
    scale = q.shape[-1] ** -0.5
    scores = torch.einsum('bhqd,bhkd->bhqk', q, k) * scale
    attn = _softmax(scores, axis=-1)
    return torch.einsum('bhqk,bhkd->bhqd', attn, v)
```

```cpp
#include <hip/hip_runtime.h>
#include <hip/hip_bf16.h>
#include <cstdint>
#include <type_traits>

constexpr int BH = 64, SKV = 16384, D = 128, NSPLIT = 4, WAVES = 4;
constexpr int GI = 2;
constexpr unsigned CHUNK = SKV / NSPLIT * D * 2;
constexpr int NG = SKV / NSPLIT / (WAVES * 64);
constexpr unsigned REGION = GI * CHUNK, LSTR = GI * WAVES * 1024, GBYTES = 16 * LSTR;
constexpr int WIN = 6;
constexpr int T0 = 90 * 16;
constexpr int TAU = 3460;
constexpr int GAIN_SH = 6;
constexpr int JUMP = 120 * 16, J0 = 30 * 16;
constexpr int LDS_BYTES = 90112;
template <int N> using IC = std::integral_constant<int, N>;

using bf16x2 = __attribute__((ext_vector_type(2))) __bf16;
using u32x4 = __attribute__((ext_vector_type(4))) unsigned;
using f32x4 = __attribute__((ext_vector_type(4))) float;
using f32x2 = __attribute__((ext_vector_type(2))) float;
typedef __attribute__((address_space(1))) unsigned long long gu64;
typedef __attribute__((address_space(1))) unsigned gu32;
typedef volatile __attribute__((address_space(3))) unsigned lu32;
#define RLX_AGENT __ATOMIC_RELAXED, __HIP_MEMORY_SCOPE_AGENT

__device__ __forceinline__ float dot2(unsigned k, unsigned q, float acc) {
  return __builtin_amdgcn_fdot2_f32_bf16(__builtin_bit_cast(bf16x2, k), __builtin_bit_cast(bf16x2, q), acc, false);
}
template <int CTRL> __device__ __forceinline__ float dpp(float x) {
  return __builtin_bit_cast(float, __builtin_amdgcn_mov_dpp(__builtin_bit_cast(int, x), CTRL, 0xf, 0xf, true));
}
constexpr int XOR1 = 0xB1, XOR2 = 0x4E, XOR7 = 0x141, XOR8 = 0x128, ROR4 = 0x124, ROR2 = 0x122, ROR1 = 0x121, BCAST = 0x150;
template <int CTRL> __device__ __forceinline__ float tmerge(float a, float b, bool hi) {
  const float ta = a + dpp<CTRL>(a), tb = b + dpp<CTRL>(b);
  return hi ? tb : ta;
}
__device__ __forceinline__ float wave_max(float x) {
  x = fmaxf(x, dpp<XOR8>(x)); x = fmaxf(x, dpp<ROR4>(x)); x = fmaxf(x, dpp<ROR2>(x)); x = fmaxf(x, dpp<ROR1>(x));
  auto s = __builtin_amdgcn_permlane16_swap(__float_as_uint(x), __float_as_uint(x), false, false);
  x = fmaxf(__uint_as_float(s[0]), __uint_as_float(s[1]));
  auto t = __builtin_amdgcn_permlane32_swap(__float_as_uint(x), __float_as_uint(x), false, false);
  return fmaxf(__uint_as_float(t[0]), __uint_as_float(t[1]));
}
__device__ __forceinline__ float wave_sum(float x) {
  x += dpp<XOR8>(x); x += dpp<ROR4>(x); x += dpp<ROR2>(x); x += dpp<ROR1>(x);
  auto s = __builtin_amdgcn_permlane16_swap(__float_as_uint(x), __float_as_uint(x), false, false);
  x = __uint_as_float(s[0]) + __uint_as_float(s[1]);
  auto t = __builtin_amdgcn_permlane32_swap(__float_as_uint(x), __float_as_uint(x), false, false);
  return __uint_as_float(t[0]) + __uint_as_float(t[1]);
}
constexpr int brev4(int u) { return ((u & 1) << 3) | ((u & 2) << 1) | ((u & 4) >> 1) | ((u & 8) >> 3); }
__device__ __forceinline__ unsigned now32() { return (unsigned)__builtin_amdgcn_s_memrealtime(); }
__device__ __forceinline__ float ex2(float x) { return __builtin_amdgcn_exp2f(x); }
__device__ __forceinline__ float finite_ref(float m) { return m == -INFINITY ? 0.f : m; }

constexpr float C2 = 0.088388347648318440f * 1.4426950408889634f;
constexpr int LDS_SC = 0, LDS_ACC = WAVES * NG * 64, LDS_MAX = LDS_ACC + WAVES * 4 * D, LDS_ST = LDS_MAX + 4;
constexpr unsigned ST_KDONE = 1, ST_LAST = 2, ST_DONE = 3;
constexpr size_t WS_EPOCH = 0, WS_GRAN = 65536, GRAN_STRIDE = 2048, WS_BYTES = WS_GRAN + (size_t)BH * NSPLIT * GRAN_STRIDE;
constexpr unsigned SPIN_CAP = 1u << 20;

#define RSRC(p, n) __builtin_amdgcn_make_buffer_rsrc((void*)(p), 0, (n), 0x00020000)
#define LD(rs, so) __builtin_amdgcn_raw_buffer_load_b128(rs, voff, (so), 2)
#define PIN(x) asm volatile("" : "+v"(x))
#define SB() __builtin_amdgcn_sched_barrier(0)
#define LDS_DRAIN() asm volatile("s_waitcnt lgkmcnt(0)" ::: "memory")

__global__ void __launch_bounds__((WAVES + 1) * 64, 2)
attn_decode(const char* __restrict__ q, const char* __restrict__ k, const char* __restrict__ v, char* __restrict__ ws,
            __hip_bfloat16* __restrict__ out) {
  extern __shared__ __attribute__((aligned(16))) float lds[];
  asm volatile("" :: "s"(q), "s"(k), "s"(v));
  const int bh = blockIdx.x / NSPLIT, split = blockIdx.x % NSPLIT;
  const int tid = threadIdx.x, lane = tid & 63, wave = __builtin_amdgcn_readfirstlane(tid >> 6);
  lu32* st = (lu32*)(lds + LDS_ST);

  asm volatile("s_cmp_lg_u32 %0, 4\n\ts_cbranch_scc1 1f\n\tds_write_b32 %1, %2\n\ts_waitcnt lgkmcnt(0)\n\ts_barrier\n1:"
               :: "s"(wave), "v"((unsigned)(uintptr_t)(st + (lane & 3))), "v"(0u) : "scc", "memory");

  if (wave == WAVES) {
    gu32* ep = (gu32*)(ws + WS_EPOCH + (size_t)bh * 128);
    const unsigned epoch = __hip_atomic_load(ep, RLX_AGENT);
    const unsigned tag = epoch * 0x9E3779B1u + 0x7F4A7C15u;
    const char* gbase = ws + WS_GRAN + (size_t)bh * NSPLIT * GRAN_STRIDE;
    auto all_waves = [&](unsigned s) { return __all(st[lane & 3] >= s); };
    auto any_wave = [&](unsigned s) { return __any(st[lane & 3] >= s); };
    while (!all_waves(ST_KDONE)) __builtin_amdgcn_s_sleep(32);
    asm volatile("" ::: "memory");
    const f32x4 mw = *reinterpret_cast<const f32x4*>(lds + LDS_MAX);
    const float M = fmaxf(fmaxf(mw[0], mw[1]), fmaxf(mw[2], mw[3])), Ms = finite_ref(M);
    float L = 0.f;
#pragma unroll 4
    for (int i = 0; i < WAVES * NG / 4; ++i) {
      const f32x4 s = *reinterpret_cast<const f32x4*>(lds + LDS_SC + i * 256 + lane * 4);
      L += (ex2((s[0] - Ms) * C2) + ex2((s[1] - Ms) * C2)) + (ex2((s[2] - Ms) * C2) + ex2((s[3] - Ms) * C2));
    }
    L = wave_sum(L);
    float f[WAVES];
#pragma unroll
    for (int w = 0; w < WAVES; ++w) f[w] = ex2((mw[w] - Ms) * C2);
    auto own_sum = [&]() {
      asm volatile("" ::: "memory");
      f32x2 t[WAVES * 4];
#pragma unroll
      for (int i = 0; i < WAVES * 4; ++i) t[i] = *reinterpret_cast<const f32x2*>(lds + LDS_ACC + i * D + 2 * lane);
      SB();
#pragma unroll
      for (int w = 0; w < WAVES; ++w) t[4 * w] = ((t[4 * w] + t[4 * w + 1]) + (t[4 * w + 2] + t[4 * w + 3])) * f[w];
      return (t[0] + t[4]) + (t[8] + t[12]);
    };
    if (split != NSPLIT - 1) {
      if (lane < 2)
        __hip_atomic_store((gu64*)(gbase + split * GRAN_STRIDE) + D + lane, ((unsigned long long)tag << 32) | __float_as_uint(lane ? L : M), RLX_AGENT);
#pragma unroll
      for (int w = 0; w < WAVES; ++w) PIN(f[w]);
      while (!any_wave(ST_LAST)) __builtin_amdgcn_s_sleep(16);
      while (!all_waves(ST_DONE)) {}
      const f32x2 s = own_sum();
      __builtin_amdgcn_raw_buffer_store_b128(u32x4{__float_as_uint(s[0]), tag, __float_as_uint(s[1]), tag},
                                             RSRC(gbase + split * GRAN_STRIDE, 1024), lane * 16, 0, 16);
    } else {
      unsigned long long x;
#pragma unroll 1
      for (unsigned spins = 0; spins < SPIN_CAP; ++spins) {
        x = __hip_atomic_load((gu64*)gbase + (lane < 6 ? (lane >> 1) * (GRAN_STRIDE / 8) + D + (lane & 1) : D), RLX_AGENT);
        if (__all((unsigned)(x >> 32) == tag)) break;
        __builtin_amdgcn_s_sleep(64);
      }
      if (lane == 0) __hip_atomic_store(ep, epoch + 1u, RLX_AGENT);
      float ms[NSPLIT], ls[NSPLIT], g[NSPLIT];
#pragma unroll
      for (int s = 0; s < NSPLIT - 1; ++s) {
        ms[s] = __uint_as_float(__builtin_amdgcn_readlane((unsigned)x, 2 * s));
        ls[s] = __uint_as_float(__builtin_amdgcn_readlane((unsigned)x, 2 * s + 1));
      }
      ms[NSPLIT - 1] = M; ls[NSPLIT - 1] = L;
      const float MM = fmaxf(fmaxf(ms[0], ms[1]), fmaxf(ms[2], ms[3]));
      float LL = 0.f;
#pragma unroll
      for (int s = 0; s < NSPLIT; ++s) { g[s] = ex2((ms[s] - MM) * C2); LL = fmaf(g[s], ls[s], LL); }
      const float inv = 1.f / LL;
#pragma unroll
      for (int s = 0; s < NSPLIT; ++s) { g[s] *= inv; PIN(g[s]); PIN(f[s]); }
      while (!any_wave(ST_LAST)) __builtin_amdgcn_s_sleep(16);
      float a0 = 0.f, a1 = 0.f, a2 = 0.f, a3 = 0.f, b0 = 0.f, b1 = 0.f, b2 = 0.f, b3 = 0.f;
      unsigned got = 0;
#define SWEEP(s) __builtin_amdgcn_raw_buffer_load_b128(RSRC(gbase + (s) * GRAN_STRIDE, (got >> (s) & 1) ? 0u : 1024u), lane * 16, 0, 16)
#define TAKE(s, p, a, b) do { const bool ok = (__all(p[1] == tag && p[3] == tag) != 0) & ((got >> (s) & 1) == 0); \
        a = ok ? __uint_as_float(p[0]) : a; b = ok ? __uint_as_float(p[2]) : b; got |= ok ? 1u << (s) : 0u; } while (0)
#pragma unroll 1
      for (unsigned spins = 0; spins < SPIN_CAP * 16 && got != 15u; ++spins) {
        asm volatile("" ::: "memory");
        const u32x4 p0 = SWEEP(0), p1 = SWEEP(1), p2 = SWEEP(2);
        SB();
        if (!(got & 8) && all_waves(ST_DONE)) { const f32x2 s = own_sum(); a3 = s[0]; b3 = s[1]; got |= 8; }
        TAKE(0, p0, a0, b0); TAKE(1, p1, a1, b1); TAKE(2, p2, a2, b2);
      }
      const bf16x2 res = {(__bf16)fmaf(g[3], a3, fmaf(g[2], a2, fmaf(g[1], a1, g[0] * a0))),
                          (__bf16)fmaf(g[3], b3, fmaf(g[2], b2, fmaf(g[1], b1, g[0] * b0)))};
      __hip_atomic_store((gu32*)out + bh * (D / 2) + lane, __builtin_bit_cast(unsigned, res), RLX_AGENT);
    }
    return;
  }

  const char* kc = k + (size_t)(blockIdx.x / GI) * REGION;
  const char* vc = v + (size_t)(blockIdx.x / GI) * REGION;
  const int c = lane & 15;
  const unsigned voff = (wave * GI + blockIdx.x % GI) * 1024 + lane * 16;
  u32x4 qv = *reinterpret_cast<const u32x4*>(q + (size_t)bh * D * 2 + c * 16);
  SB();
  u32x4 Rc[16], Rn[16];
  {
    const auto rk = RSRC(kc, REGION);
#pragma unroll
    for (int u = 0; u < WIN; ++u) Rc[u] = LD(rk, u * LSTR);
  }
  SB();
  unsigned anchor = now32() * 16u + T0;
  __builtin_amdgcn_s_barrier();
  SB();
  PIN(qv);
  float* sc = lds + LDS_SC + wave * (NG * 64) + lane;
  const bool b8 = c & 8, b4 = c & 4, b2 = c & 2, b1 = c & 1;

#define ISSUE(j) do { if ((j) < 16) Rc[(j) & 15] = LD(rc, soc + (j) * LSTR); else Rn[(j) & 15] = LD(rn, son + ((j) - 16) * LSTR); } while (0)
#define TIGHT(lv, thr, w) asm volatile("s_cmp_gt_i32 %0, %1\n\ts_cbranch_scc1 1f\n\ts_waitcnt vmcnt(%2)\n1:" :: "s"(lv), "n"(thr), "n"((w) - 1) : "scc")
#define WINDOW(u) do { PIN(Rc[u]); SB(); \
    if (LAST) { if ((u) == 0) { _Pragma("unroll") for (int j = WIN; j < 16; ++j) ISSUE(j); } } \
    else { TIGHT(level, brev4(u), WIN - 1); SB(); ISSUE((u) + WIN); } \
    SB(); \
    if ((u) % 8 == 0 && !LAST) { tnow = now32(); SB(); } \
    if ((u) % 8 == 2 && !LAST) { \
      int lt = (int)(tnow * 16u - anchor); \
      if (lt > cap) { anchor += lt - late; lt = late; } \
      late = lt; cap = lt + JUMP; level = lt >> GAIN_SH; anchor += TAU / 2; \
      SB(); } \
    } while (0)
#define STEPS(body) do { body(IC<0>{}); body(IC<1>{}); body(IC<2>{}); body(IC<3>{}); body(IC<4>{}); body(IC<5>{}); body(IC<6>{}); body(IC<7>{}); \
    body(IC<8>{}); body(IC<9>{}); body(IC<10>{}); body(IC<11>{}); body(IC<12>{}); body(IC<13>{}); body(IC<14>{}); body(IC<15>{}); } while (0)
  int level = 0, late = J0, cap = J0;
  unsigned tnow = 0;

  float m = -INFINITY;
#pragma unroll 1
  for (int g = 0; g < NG; ++g) {
    constexpr bool LAST = false;
    const auto rc = RSRC(kc, REGION);
    const auto rn = RSRC(g + 1 < NG ? kc : vc, REGION);
    const unsigned soc = g * GBYTES, son = g + 1 < NG ? soc + GBYTES : 0u;
    float x[16];
    auto body = [&](auto uc) {
      constexpr int u = decltype(uc)::value;
      WINDOW(u);
      const u32x4 kk = Rc[u];
      float d = dot2(kk[0], qv[0], 0.f);
      d = dot2(kk[1], qv[1], d);
      d = dot2(kk[2], qv[2], d);
      x[u] = dot2(kk[3], qv[3], d);
      if constexpr (u & 1) {
        x[u] = tmerge<XOR8>(x[u - 1], x[u], b8);
        if constexpr (u & 2) {
          x[u] = tmerge<XOR7>(x[u - 2], x[u], b4);
          if constexpr (u & 4) {
            x[u] = tmerge<XOR2>(x[u - 4], x[u], b2);
            if constexpr (u & 8) x[u] = tmerge<XOR1>(x[u - 8], x[u], b1);
          }
        }
      }
      PIN(x[u]);
    };
    STEPS(body);
    sc[g * 64] = x[15];
    m = fmaxf(m, x[15]);
#pragma unroll
    for (int u = 0; u < WIN; ++u) Rc[u] = Rn[u];
  }
  float snext = sc[0];
  m = wave_max(m);
  lds[LDS_MAX + wave] = m;
  LDS_DRAIN();
  st[wave] = ST_KDONE;
  m = finite_ref(m);

  f32x2 acc[4];
#pragma unroll
  for (int i = 0; i < 4; ++i) acc[i] = f32x2{0.f, 0.f};
  auto vgroup = [&](int g, auto last) {
    constexpr bool LAST = decltype(last)::value;
    const auto rc = RSRC(vc, REGION);
    const auto rn = rc;
    const unsigned soc = g * GBYTES, son = soc + GBYTES;
    float p;
    auto body = [&](auto uc) {
      constexpr int u = decltype(uc)::value;
      WINDOW(u);
      if constexpr (u == 0) {
        p = ex2((snext - m) * C2);
        if (!LAST) snext = sc[(g + 1) * 64];
      }
      const u32x4 vv = Rc[u];
      const float pb = dpp<BCAST + brev4(u)>(p);
      const f32x2 pp = {pb, pb};
#pragma unroll
      for (int i = 0; i < 4; ++i) {
        const f32x2 vf = {__builtin_bit_cast(float, vv[i] << 16), __builtin_bit_cast(float, vv[i] & 0xffff0000u)};
        acc[i] = __builtin_elementwise_fma(pp, vf, acc[i]);
      }
      PIN(acc[0]); PIN(acc[1]); PIN(acc[2]); PIN(acc[3]);
    };
    STEPS(body);
    if (!LAST) {
#pragma unroll
      for (int u = 0; u < WIN; ++u) Rc[u] = Rn[u];
    }
  };
#pragma unroll 1
  for (int g = 0; g < NG - 1; ++g) vgroup(g, IC<0>{});
  st[wave] = ST_LAST;
  vgroup(NG - 1, IC<1>{});
  float* dst = lds + LDS_ACC + wave * (4 * D) + lane * 8;
  *reinterpret_cast<f32x4*>(dst) = f32x4{acc[0][0], acc[0][1], acc[1][0], acc[1][1]};
  *reinterpret_cast<f32x4*>(dst + 4) = f32x4{acc[2][0], acc[2][1], acc[3][0], acc[3][1]};
  LDS_DRAIN();
  st[wave] = ST_DONE;
}

extern "C" void kernel_launch(void* const* d_in, const int* in_sizes, int n_in, void* d_out, int out_size, void* d_ws,
                              size_t ws_size, hipStream_t stream) {
  if (n_in != 3 || in_sizes[0] != BH * D || in_sizes[1] != BH * SKV * D || in_sizes[2] != BH * SKV * D || out_size != BH * D ||
      ws_size < WS_BYTES)
    return;
  (void)hipFuncSetAttribute((const void*)attn_decode, hipFuncAttributeMaxDynamicSharedMemorySize, LDS_BYTES);
  attn_decode<<<BH * NSPLIT, (WAVES + 1) * 64, LDS_BYTES, stream>>>((const char*)d_in[0], (const char*)d_in[1], (const char*)d_in[2],
                                                                     (char*)d_ws, (__hip_bfloat16*)d_out);
}
```

```cpp
#include <hip/hip_runtime.h>
#include <hip/hip_bf16.h>
#include <cstdint>
#include <type_traits>

constexpr int BH = 64, SKV = 16384, D = 128, NSPLIT = 4, WAVES = 4;
constexpr int GI = 2;
constexpr unsigned CHUNK = SKV / NSPLIT * D * 2;
constexpr int NG = SKV / NSPLIT / (WAVES * 64);
constexpr unsigned REGION = GI * CHUNK, LSTR = GI * WAVES * 1024, GBYTES = 16 * LSTR;
constexpr int WIN = 8;
constexpr int WHI = 6;
constexpr int T0 = 100 * 16;
constexpr int TAU = 3445;
constexpr int GAIN_SH = 6;
constexpr int JUMP = 120 * 16, J0 = 30 * 16;
constexpr int LDS_BYTES = 90112;
template <int N> using IC = std::integral_constant<int, N>;

using bf16x2 = __attribute__((ext_vector_type(2))) __bf16;
using u32x4 = __attribute__((ext_vector_type(4))) unsigned;
using f32x4 = __attribute__((ext_vector_type(4))) float;
using f32x2 = __attribute__((ext_vector_type(2))) float;
typedef __attribute__((address_space(1))) unsigned long long gu64;
typedef __attribute__((address_space(1))) unsigned gu32;
typedef volatile __attribute__((address_space(3))) unsigned lu32;
#define RLX_AGENT __ATOMIC_RELAXED, __HIP_MEMORY_SCOPE_AGENT

__device__ __forceinline__ float dot2(unsigned k, unsigned q, float acc) {
  return __builtin_amdgcn_fdot2_f32_bf16(__builtin_bit_cast(bf16x2, k), __builtin_bit_cast(bf16x2, q), acc, false);
}
template <int CTRL> __device__ __forceinline__ float dpp(float x) {
  return __builtin_bit_cast(float, __builtin_amdgcn_mov_dpp(__builtin_bit_cast(int, x), CTRL, 0xf, 0xf, true));
}
constexpr int XOR1 = 0xB1, XOR2 = 0x4E, XOR7 = 0x141, XOR8 = 0x128, ROR4 = 0x124, ROR2 = 0x122, ROR1 = 0x121, BCAST = 0x150;
template <int CTRL> __device__ __forceinline__ float tmerge(float a, float b, bool hi) {
  const float ta = a + dpp<CTRL>(a), tb = b + dpp<CTRL>(b);
  return hi ? tb : ta;
}
__device__ __forceinline__ float wave_max(float x) {
  x = fmaxf(x, dpp<XOR8>(x)); x = fmaxf(x, dpp<ROR4>(x)); x = fmaxf(x, dpp<ROR2>(x)); x = fmaxf(x, dpp<ROR1>(x));
  auto s = __builtin_amdgcn_permlane16_swap(__float_as_uint(x), __float_as_uint(x), false, false);
  x = fmaxf(__uint_as_float(s[0]), __uint_as_float(s[1]));
  auto t = __builtin_amdgcn_permlane32_swap(__float_as_uint(x), __float_as_uint(x), false, false);
  return fmaxf(__uint_as_float(t[0]), __uint_as_float(t[1]));
}
__device__ __forceinline__ float wave_sum(float x) {
  x += dpp<XOR8>(x); x += dpp<ROR4>(x); x += dpp<ROR2>(x); x += dpp<ROR1>(x);
  auto s = __builtin_amdgcn_permlane16_swap(__float_as_uint(x), __float_as_uint(x), false, false);
  x = __uint_as_float(s[0]) + __uint_as_float(s[1]);
  auto t = __builtin_amdgcn_permlane32_swap(__float_as_uint(x), __float_as_uint(x), false, false);
  return __uint_as_float(t[0]) + __uint_as_float(t[1]);
}
constexpr int brev4(int u) { return ((u & 1) << 3) | ((u & 2) << 1) | ((u & 4) >> 1) | ((u & 8) >> 3); }
__device__ __forceinline__ unsigned now32() { return (unsigned)__builtin_amdgcn_s_memrealtime(); }
__device__ __forceinline__ float ex2(float x) { return __builtin_amdgcn_exp2f(x); }
__device__ __forceinline__ float finite_ref(float m) { return m == -INFINITY ? 0.f : m; }

constexpr float C2 = 0.088388347648318440f * 1.4426950408889634f;
constexpr int LDS_SC = 0, LDS_ACC = WAVES * NG * 64, LDS_MAX = LDS_ACC + WAVES * 4 * D, LDS_ST = LDS_MAX + 4;
constexpr unsigned ST_KDONE = 1, ST_LAST = 2, ST_DONE = 3;
constexpr size_t WS_EPOCH = 0, WS_GRAN = 65536, GRAN_STRIDE = 2048, WS_BYTES = WS_GRAN + (size_t)BH * NSPLIT * GRAN_STRIDE;
constexpr unsigned SPIN_CAP = 1u << 20;

#define RSRC(p, n) __builtin_amdgcn_make_buffer_rsrc((void*)(p), 0, (n), 0x00020000)
#define LD(rs, so) __builtin_amdgcn_raw_buffer_load_b128(rs, voff, (so), 2)
#define PIN(x) asm volatile("" : "+v"(x))
#define SB() __builtin_amdgcn_sched_barrier(0)
#define LDS_DRAIN() asm volatile("s_waitcnt lgkmcnt(0)" ::: "memory")

__global__ void __launch_bounds__((WAVES + 1) * 64, 2)
attn_decode(const char* __restrict__ q, const char* __restrict__ k, const char* __restrict__ v, char* __restrict__ ws,
            __hip_bfloat16* __restrict__ out) {
  extern __shared__ __attribute__((aligned(16))) float lds[];
  asm volatile("" :: "s"(q), "s"(k), "s"(v));
  const int bh = blockIdx.x / NSPLIT, split = blockIdx.x % NSPLIT;
  const int tid = threadIdx.x, lane = tid & 63, wave = __builtin_amdgcn_readfirstlane(tid >> 6);
  lu32* st = (lu32*)(lds + LDS_ST);

  asm volatile("s_cmp_lg_u32 %0, 4\n\ts_cbranch_scc1 1f\n\tds_write_b32 %1, %2\n\ts_waitcnt lgkmcnt(0)\n\ts_barrier\n1:"
               :: "s"(wave), "v"((unsigned)(uintptr_t)(st + (lane & 3))), "v"(0u) : "scc", "memory");

  if (wave == WAVES) {
    gu32* ep = (gu32*)(ws + WS_EPOCH + (size_t)bh * 128);
    const unsigned epoch = __hip_atomic_load(ep, RLX_AGENT);
    const unsigned tag = epoch * 0x9E3779B1u + 0x7F4A7C15u;
    const char* gbase = ws + WS_GRAN + (size_t)bh * NSPLIT * GRAN_STRIDE;
    auto all_waves = [&](unsigned s) { return __all(st[lane & 3] >= s); };
    auto any_wave = [&](unsigned s) { return __any(st[lane & 3] >= s); };
    while (!all_waves(ST_KDONE)) __builtin_amdgcn_s_sleep(32);
    asm volatile("" ::: "memory");
    const f32x4 mw = *reinterpret_cast<const f32x4*>(lds + LDS_MAX);
    const float M = fmaxf(fmaxf(mw[0], mw[1]), fmaxf(mw[2], mw[3])), Ms = finite_ref(M);
    float L = 0.f;
#pragma unroll 4
    for (int i = 0; i < WAVES * NG / 4; ++i) {
      const f32x4 s = *reinterpret_cast<const f32x4*>(lds + LDS_SC + i * 256 + lane * 4);
      L += (ex2((s[0] - Ms) * C2) + ex2((s[1] - Ms) * C2)) + (ex2((s[2] - Ms) * C2) + ex2((s[3] - Ms) * C2));
    }
    L = wave_sum(L);
    float f[WAVES];
#pragma unroll
    for (int w = 0; w < WAVES; ++w) f[w] = ex2((mw[w] - Ms) * C2);
    auto own_sum = [&]() {
      asm volatile("" ::: "memory");
      f32x2 t[WAVES * 4];
#pragma unroll
      for (int i = 0; i < WAVES * 4; ++i) t[i] = *reinterpret_cast<const f32x2*>(lds + LDS_ACC + i * D + 2 * lane);
      SB();
#pragma unroll
      for (int w = 0; w < WAVES; ++w) t[4 * w] = ((t[4 * w] + t[4 * w + 1]) + (t[4 * w + 2] + t[4 * w + 3])) * f[w];
      return (t[0] + t[4]) + (t[8] + t[12]);
    };
    if (split != NSPLIT - 1) {
      if (lane < 16)
        __hip_atomic_store((gu64*)(gbase + split * GRAN_STRIDE) + D + lane, ((unsigned long long)tag << 32) | __float_as_uint(lane & 1 ? L : M), RLX_AGENT);
#pragma unroll
      for (int w = 0; w < WAVES; ++w) PIN(f[w]);
      while (!any_wave(ST_LAST)) __builtin_amdgcn_s_sleep(16);
      while (!all_waves(ST_DONE)) {}
      const f32x2 s = own_sum();
      __builtin_amdgcn_raw_buffer_store_b128(u32x4{__float_as_uint(s[0]), tag, __float_as_uint(s[1]), tag},
                                             RSRC(gbase + split * GRAN_STRIDE, 1024), lane * 16, 0, 16);
    } else {
      __builtin_amdgcn_s_sleep(127); __builtin_amdgcn_s_sleep(127);
      unsigned long long x;
#pragma unroll 1
      for (unsigned spins = 0; spins < SPIN_CAP; ++spins) {
        x = __hip_atomic_load((gu64*)gbase + (lane < 6 ? (lane >> 1) * (GRAN_STRIDE / 8) + D + (lane & 1) : D), RLX_AGENT);
        if (__all((unsigned)(x >> 32) == tag)) break;
        __builtin_amdgcn_s_sleep(64);
      }
      if (lane < 32) __hip_atomic_store(ep + lane, epoch + 1u, RLX_AGENT);
      float ms[NSPLIT], ls[NSPLIT], g[NSPLIT];
#pragma unroll
      for (int s = 0; s < NSPLIT - 1; ++s) {
        ms[s] = __uint_as_float(__builtin_amdgcn_readlane((unsigned)x, 2 * s));
        ls[s] = __uint_as_float(__builtin_amdgcn_readlane((unsigned)x, 2 * s + 1));
      }
      ms[NSPLIT - 1] = M; ls[NSPLIT - 1] = L;
      const float MM = fmaxf(fmaxf(ms[0], ms[1]), fmaxf(ms[2], ms[3]));
      float LL = 0.f;
#pragma unroll
      for (int s = 0; s < NSPLIT; ++s) { g[s] = ex2((ms[s] - MM) * C2); LL = fmaf(g[s], ls[s], LL); }
      const float inv = 1.f / LL;
#pragma unroll
      for (int s = 0; s < NSPLIT; ++s) { g[s] *= inv; PIN(g[s]); PIN(f[s]); }
      while (!any_wave(ST_LAST)) __builtin_amdgcn_s_sleep(16);
      float a0 = 0.f, a1 = 0.f, a2 = 0.f, a3 = 0.f, b0 = 0.f, b1 = 0.f, b2 = 0.f, b3 = 0.f;
      unsigned got = 0;
#define SWEEP(s) __builtin_amdgcn_raw_buffer_load_b128(RSRC(gbase + (s) * GRAN_STRIDE, (got >> (s) & 1) ? 0u : 1024u), lane * 16, 0, 16)
#define TAKE(s, p, a, b) do { const bool ok = (__all(p[1] == tag && p[3] == tag) != 0) & ((got >> (s) & 1) == 0); \
        a = ok ? __uint_as_float(p[0]) : a; b = ok ? __uint_as_float(p[2]) : b; got |= ok ? 1u << (s) : 0u; } while (0)
#pragma unroll 1
      for (unsigned spins = 0; spins < SPIN_CAP * 16 && got != 15u; ++spins) {
        asm volatile("" ::: "memory");
        const u32x4 p0 = SWEEP(0), p1 = SWEEP(1), p2 = SWEEP(2);
        SB();
        if (!(got & 8) && all_waves(ST_DONE)) { const f32x2 s = own_sum(); a3 = s[0]; b3 = s[1]; got |= 8; }
        TAKE(0, p0, a0, b0); TAKE(1, p1, a1, b1); TAKE(2, p2, a2, b2);
      }
      const bf16x2 res = {(__bf16)fmaf(g[3], a3, fmaf(g[2], a2, fmaf(g[1], a1, g[0] * a0))),
                          (__bf16)fmaf(g[3], b3, fmaf(g[2], b2, fmaf(g[1], b1, g[0] * b0)))};
      __hip_atomic_store((gu32*)out + bh * (D / 2) + lane, __builtin_bit_cast(unsigned, res), RLX_AGENT);
    }
    return;
  }

  const char* kc = k + (size_t)(blockIdx.x / GI) * REGION;
  const char* vc = v + (size_t)(blockIdx.x / GI) * REGION;
  const int c = lane & 15;
  const unsigned voff = (wave * GI + blockIdx.x % GI) * 1024 + lane * 16;
  u32x4 qv = *reinterpret_cast<const u32x4*>(q + (size_t)bh * D * 2 + c * 16);
  SB();
  u32x4 Rc[16], Rn[16];
  {
    const auto rk = RSRC(kc, REGION);
#pragma unroll
    for (int u = 0; u < WIN; ++u) Rc[u] = LD(rk, u * LSTR);
  }
  SB();
  unsigned anchor = now32() * 16u + T0;
  __builtin_amdgcn_s_barrier();
  SB();
  PIN(qv);
  float* sc = lds + LDS_SC + wave * (NG * 64) + lane;
  const bool b8 = c & 8, b4 = c & 4, b2 = c & 2, b1 = c & 1;

#define ISSUE(j) do { if ((j) < 16) Rc[(j) & 15] = LD(rc, soc + (j) * LSTR); else Rn[(j) & 15] = LD(rn, son + ((j) - 16) * LSTR); } while (0)
#define TIGHT(lv, thr) asm volatile("s_waitcnt vmcnt(%2)\n\ts_cmp_gt_i32 %0, %1\n\ts_cbranch_scc1 1f\n\ts_waitcnt vmcnt(%3)\n1:" \
                                    :: "s"(lv), "n"(thr), "n"(WHI - 1), "n"(WHI - 2) : "scc")
#define WINDOW(u) do { PIN(Rc[u]); SB(); \
    if (LAST) { if ((u) == 0) { _Pragma("unroll") for (int j = WIN; j < 16; ++j) ISSUE(j); } } \
    else { TIGHT(level, brev4(u)); SB(); ISSUE((u) + WIN); } \
    SB(); \
    if ((u) % 8 == 0 && !LAST) { tnow = now32(); SB(); } \
    if ((u) % 8 == 2 && !LAST) { \
      int lt = (int)(tnow * 16u - anchor); \
      if (lt > cap) { anchor += lt - late; lt = late; } \
      late = lt; cap = lt + JUMP; level = lt >> GAIN_SH; anchor += TAU / 2; \
      SB(); } \
    } while (0)
#define STEPS(body) do { body(IC<0>{}); body(IC<1>{}); body(IC<2>{}); body(IC<3>{}); body(IC<4>{}); body(IC<5>{}); body(IC<6>{}); body(IC<7>{}); \
    body(IC<8>{}); body(IC<9>{}); body(IC<10>{}); body(IC<11>{}); body(IC<12>{}); body(IC<13>{}); body(IC<14>{}); body(IC<15>{}); } while (0)
  int level = 0, late = J0, cap = J0;
  unsigned tnow = 0;

  float m = -INFINITY;
#pragma unroll 1
  for (int g = 0; g < NG; ++g) {
    constexpr bool LAST = false;
    const auto rc = RSRC(kc, REGION);
    const auto rn = RSRC(g + 1 < NG ? kc : vc, REGION);
    const unsigned soc = g * GBYTES, son = g + 1 < NG ? soc + GBYTES : 0u;
    float x[16];
    auto body = [&](auto uc) {
      constexpr int u = decltype(uc)::value;
      WINDOW(u);
      const u32x4 kk = Rc[u];
      float d = dot2(kk[0], qv[0], 0.f);
      d = dot2(kk[1], qv[1], d);
      d = dot2(kk[2], qv[2], d);
      x[u] = dot2(kk[3], qv[3], d);
      if constexpr (u & 1) {
        x[u] = tmerge<XOR8>(x[u - 1], x[u], b8);
        if constexpr (u & 2) {
          x[u] = tmerge<XOR7>(x[u - 2], x[u], b4);
          if constexpr (u & 4) {
            x[u] = tmerge<XOR2>(x[u - 4], x[u], b2);
            if constexpr (u & 8) x[u] = tmerge<XOR1>(x[u - 8], x[u], b1);
          }
        }
      }
      PIN(x[u]);
    };
    STEPS(body);
    sc[g * 64] = x[15];
    m = fmaxf(m, x[15]);
#pragma unroll
    for (int u = 0; u < WIN; ++u) Rc[u] = Rn[u];
  }
  float snext = sc[0];
  m = wave_max(m);
  lds[LDS_MAX + wave] = m;
  LDS_DRAIN();
  st[wave] = ST_KDONE;
  m = finite_ref(m);

  f32x2 acc[4];
#pragma unroll
  for (int i = 0; i < 4; ++i) acc[i] = f32x2{0.f, 0.f};
  auto vgroup = [&](int g, auto last) {
    constexpr bool LAST = decltype(last)::value;
    const auto rc = RSRC(vc, REGION);
    const auto rn = rc;
    const unsigned soc = g * GBYTES, son = soc + GBYTES;
    float p;
    auto body = [&](auto uc) {
      constexpr int u = decltype(uc)::value;
      WINDOW(u);
      if constexpr (u == 0) {
        p = ex2((snext - m) * C2);
        if (!LAST) snext = sc[(g + 1) * 64];
      }
      const u32x4 vv = Rc[u];
      const float pb = dpp<BCAST + brev4(u)>(p);
      const f32x2 pp = {pb, pb};
#pragma unroll
      for (int i = 0; i < 4; ++i) {
        const f32x2 vf = {__builtin_bit_cast(float, vv[i] << 16), __builtin_bit_cast(float, vv[i] & 0xffff0000u)};
        acc[i] = __builtin_elementwise_fma(pp, vf, acc[i]);
      }
      PIN(acc[0]); PIN(acc[1]); PIN(acc[2]); PIN(acc[3]);
    };
    STEPS(body);
    if (!LAST) {
#pragma unroll
      for (int u = 0; u < WIN; ++u) Rc[u] = Rn[u];
    }
  };
#pragma unroll 1
  for (int g = 0; g < NG - 1; ++g) vgroup(g, IC<0>{});
  st[wave] = ST_LAST;
  vgroup(NG - 1, IC<1>{});
  float* dst = lds + LDS_ACC + wave * (4 * D) + lane * 8;
  *reinterpret_cast<f32x4*>(dst) = f32x4{acc[0][0], acc[0][1], acc[1][0], acc[1][1]};
  *reinterpret_cast<f32x4*>(dst + 4) = f32x4{acc[2][0], acc[2][1], acc[3][0], acc[3][1]};
  LDS_DRAIN();
  st[wave] = ST_DONE;
}

extern "C" void kernel_launch(void* const* d_in, const int* in_sizes, int n_in, void* d_out, int out_size, void* d_ws,
                              size_t ws_size, hipStream_t stream) {
  if (n_in != 3 || in_sizes[0] != BH * D || in_sizes[1] != BH * SKV * D || in_sizes[2] != BH * SKV * D || out_size != BH * D ||
      ws_size < WS_BYTES)
    return;
  (void)hipFuncSetAttribute((const void*)attn_decode, hipFuncAttributeMaxDynamicSharedMemorySize, LDS_BYTES);
  attn_decode<<<BH * NSPLIT, (WAVES + 1) * 64, LDS_BYTES, stream>>>((const char*)d_in[0], (const char*)d_in[1], (const char*)d_in[2],
                                                                     (char*)d_ws, (__hip_bfloat16*)d_out);
}
```

```cpp
#include <hip/hip_runtime.h>
#include <hip/hip_bf16.h>
#include <cstdint>
#include <type_traits>

constexpr int BH = 64, SKV = 16384, D = 128, NSPLIT = 4, WAVES = 4;
constexpr int GI = 2;
constexpr unsigned CHUNK = SKV / NSPLIT * D * 2;
constexpr int NG = SKV / NSPLIT / (WAVES * 64);
constexpr unsigned REGION = GI * CHUNK, LSTR = GI * WAVES * 1024, GBYTES = 16 * LSTR;
constexpr int WIN = 8;
constexpr int WHI = 6;
constexpr int T0 = 100 * 16;
constexpr int TAU = 3445;
constexpr int GAIN_SH = 6;
constexpr int JUMP = 120 * 16, J0 = 30 * 16;
constexpr int LDS_BYTES = 90112;
template <int N> using IC = std::integral_constant<int, N>;

using bf16x2 = __attribute__((ext_vector_type(2))) __bf16;
using u32x4 = __attribute__((ext_vector_type(4))) unsigned;
using f32x4 = __attribute__((ext_vector_type(4))) float;
using f32x2 = __attribute__((ext_vector_type(2))) float;
typedef __attribute__((address_space(1))) unsigned long long gu64;
typedef __attribute__((address_space(1))) unsigned gu32;
typedef volatile __attribute__((address_space(3))) unsigned lu32;
#define RLX_AGENT __ATOMIC_RELAXED, __HIP_MEMORY_SCOPE_AGENT

__device__ __forceinline__ float dot2(unsigned k, unsigned q, float acc) {
  return __builtin_amdgcn_fdot2_f32_bf16(__builtin_bit_cast(bf16x2, k), __builtin_bit_cast(bf16x2, q), acc, false);
}
template <int CTRL> __device__ __forceinline__ float dpp(float x) {
  return __builtin_bit_cast(float, __builtin_amdgcn_mov_dpp(__builtin_bit_cast(int, x), CTRL, 0xf, 0xf, true));
}
constexpr int XOR1 = 0xB1, XOR2 = 0x4E, XOR7 = 0x141, XOR8 = 0x128, ROR4 = 0x124, ROR2 = 0x122, ROR1 = 0x121, BCAST = 0x150;
template <int CTRL> __device__ __forceinline__ float tmerge(float a, float b, bool hi) {
  const float ta = a + dpp<CTRL>(a), tb = b + dpp<CTRL>(b);
  return hi ? tb : ta;
}
__device__ __forceinline__ float wave_max(float x) {
  x = fmaxf(x, dpp<XOR8>(x)); x = fmaxf(x, dpp<ROR4>(x)); x = fmaxf(x, dpp<ROR2>(x)); x = fmaxf(x, dpp<ROR1>(x));
  auto s = __builtin_amdgcn_permlane16_swap(__float_as_uint(x), __float_as_uint(x), false, false);
  x = fmaxf(__uint_as_float(s[0]), __uint_as_float(s[1]));
  auto t = __builtin_amdgcn_permlane32_swap(__float_as_uint(x), __float_as_uint(x), false, false);
  return fmaxf(__uint_as_float(t[0]), __uint_as_float(t[1]));
}
__device__ __forceinline__ float wave_sum(float x) {
  x += dpp<XOR8>(x); x += dpp<ROR4>(x); x += dpp<ROR2>(x); x += dpp<ROR1>(x);
  auto s = __builtin_amdgcn_permlane16_swap(__float_as_uint(x), __float_as_uint(x), false, false);
  x = __uint_as_float(s[0]) + __uint_as_float(s[1]);
  auto t = __builtin_amdgcn_permlane32_swap(__float_as_uint(x), __float_as_uint(x), false, false);
  return __uint_as_float(t[0]) + __uint_as_float(t[1]);
}
constexpr int brev4(int u) { return ((u & 1) << 3) | ((u & 2) << 1) | ((u & 4) >> 1) | ((u & 8) >> 3); }
__device__ __forceinline__ unsigned now32() { return (unsigned)__builtin_amdgcn_s_memrealtime(); }
__device__ __forceinline__ float ex2(float x) { return __builtin_amdgcn_exp2f(x); }
__device__ __forceinline__ float finite_ref(float m) { return m == -INFINITY ? 0.f : m; }

constexpr float C2 = 0.088388347648318440f * 1.4426950408889634f;
constexpr int LDS_SC = 0, LDS_ACC = WAVES * NG * 64, LDS_MAX = LDS_ACC + WAVES * 4 * D, LDS_ST = LDS_MAX + 4;
constexpr unsigned ST_KDONE = 1, ST_LAST = 2, ST_DONE = 3;
constexpr size_t WS_EPOCH = 0, WS_GRAN = 65536, GRAN_STRIDE = 2048, WS_BYTES = WS_GRAN + (size_t)BH * NSPLIT * GRAN_STRIDE;
constexpr unsigned SPIN_CAP = 1u << 18;

#define RSRC(p, n) __builtin_amdgcn_make_buffer_rsrc((void*)(p), 0, (n), 0x00020000)
#define LD(rs, so) __builtin_amdgcn_raw_buffer_load_b128(rs, voff, (so), 2)
#define PIN(x) asm volatile("" : "+v"(x))
#define SB() __builtin_amdgcn_sched_barrier(0)
#define LDS_DRAIN() asm volatile("s_waitcnt lgkmcnt(0)" ::: "memory")

__global__ void __launch_bounds__((WAVES + 1) * 64, 2)
attn_decode(const char* __restrict__ q, const char* __restrict__ k, const char* __restrict__ v, char* __restrict__ ws,
            __hip_bfloat16* __restrict__ out) {
  extern __shared__ __attribute__((aligned(16))) float lds[];
  asm volatile("" :: "s"(q), "s"(k), "s"(v));
  const int bh = blockIdx.x / NSPLIT, split = blockIdx.x % NSPLIT;
  const int tid = threadIdx.x, lane = tid & 63, wave = __builtin_amdgcn_readfirstlane(tid >> 6);
  lu32* st = (lu32*)(lds + LDS_ST);

  asm volatile("s_cmp_lg_u32 %0, 4\n\ts_cbranch_scc1 1f\n\tds_write_b32 %1, %2\n\ts_waitcnt lgkmcnt(0)\n\ts_barrier\n1:"
               :: "s"(wave), "v"((unsigned)(uintptr_t)(st + (lane & 3))), "v"(0u) : "scc", "memory");

  if (wave == WAVES) {
    gu32* ep = (gu32*)(ws + WS_EPOCH + (size_t)bh * 128);
    const unsigned epoch = __hip_atomic_load(ep, RLX_AGENT);
    const unsigned tag = epoch * 0x9E3779B1u + 0x7F4A7C15u;
    const char* gbase = ws + WS_GRAN + (size_t)bh * NSPLIT * GRAN_STRIDE;
    auto all_waves = [&](unsigned s) { return __all(st[lane & 3] >= s); };
    auto any_wave = [&](unsigned s) { return __any(st[lane & 3] >= s); };
    while (!all_waves(ST_KDONE)) __builtin_amdgcn_s_sleep(32);
    asm volatile("" ::: "memory");
    const f32x4 mw = *reinterpret_cast<const f32x4*>(lds + LDS_MAX);
    const float M = fmaxf(fmaxf(mw[0], mw[1]), fmaxf(mw[2], mw[3])), Ms = finite_ref(M);
    float L = 0.f;
#pragma unroll 4
    for (int i = 0; i < WAVES * NG / 4; ++i) {
      const f32x4 s = *reinterpret_cast<const f32x4*>(lds + LDS_SC + i * 256 + lane * 4);
      L += (ex2((s[0] - Ms) * C2) + ex2((s[1] - Ms) * C2)) + (ex2((s[2] - Ms) * C2) + ex2((s[3] - Ms) * C2));
    }
    L = wave_sum(L);
    float f[WAVES];
#pragma unroll
    for (int w = 0; w < WAVES; ++w) f[w] = ex2((mw[w] - Ms) * C2);
    auto own_sum = [&]() {
      asm volatile("" ::: "memory");
      f32x2 t[WAVES * 4];
#pragma unroll
      for (int i = 0; i < WAVES * 4; ++i) t[i] = *reinterpret_cast<const f32x2*>(lds + LDS_ACC + i * D + 2 * lane);
      SB();
#pragma unroll
      for (int w = 0; w < WAVES; ++w) t[4 * w] = ((t[4 * w] + t[4 * w + 1]) + (t[4 * w + 2] + t[4 * w + 3])) * f[w];
      return (t[0] + t[4]) + (t[8] + t[12]);
    };
    if (split != NSPLIT - 1) {
      if (lane < 16)
        __hip_atomic_store((gu64*)(gbase + split * GRAN_STRIDE) + D + lane, ((unsigned long long)tag << 32) | __float_as_uint(lane & 1 ? L : M), RLX_AGENT);
#pragma unroll
      for (int w = 0; w < WAVES; ++w) PIN(f[w]);
      while (!any_wave(ST_LAST)) __builtin_amdgcn_s_sleep(16);
      while (!all_waves(ST_DONE)) {}
      const f32x2 s = own_sum();
      __builtin_amdgcn_raw_buffer_store_b128(u32x4{__float_as_uint(s[0]), tag, __float_as_uint(s[1]), tag},
                                             RSRC(gbase + split * GRAN_STRIDE, 1024), lane * 16, 0, 16);
    } else {
      __builtin_amdgcn_s_sleep(127); __builtin_amdgcn_s_sleep(127);
      unsigned long long x;
#pragma unroll 1
      for (unsigned spins = 0; spins < SPIN_CAP; ++spins) {
        x = __hip_atomic_load((gu64*)gbase + (lane < 6 ? (lane >> 1) * (GRAN_STRIDE / 8) + D + (lane & 1) : D), RLX_AGENT);
        if (__all((unsigned)(x >> 32) == tag)) break;
        __builtin_amdgcn_s_sleep(64);
      }
      if (lane < 32) __hip_atomic_store(ep + lane, epoch + 1u, RLX_AGENT);
      float ms[NSPLIT], ls[NSPLIT], g[NSPLIT];
#pragma unroll
      for (int s = 0; s < NSPLIT - 1; ++s) {
        ms[s] = __uint_as_float(__builtin_amdgcn_readlane((unsigned)x, 2 * s));
        ls[s] = __uint_as_float(__builtin_amdgcn_readlane((unsigned)x, 2 * s + 1));
      }
      ms[NSPLIT - 1] = M; ls[NSPLIT - 1] = L;
      const float MM = fmaxf(fmaxf(ms[0], ms[1]), fmaxf(ms[2], ms[3]));
      float LL = 0.f;
#pragma unroll
      for (int s = 0; s < NSPLIT; ++s) { g[s] = ex2((ms[s] - MM) * C2); LL = fmaf(g[s], ls[s], LL); }
      const float inv = __all((unsigned)(x >> 32) == tag) ? 1.f / LL : __builtin_nanf("");
#pragma unroll
      for (int s = 0; s < NSPLIT; ++s) { g[s] *= inv; PIN(g[s]); PIN(f[s]); }
      while (!any_wave(ST_LAST)) __builtin_amdgcn_s_sleep(16);
      float a0 = 0.f, a1 = 0.f, a2 = 0.f, a3 = 0.f, b0 = 0.f, b1 = 0.f, b2 = 0.f, b3 = 0.f;
      unsigned got = 0;
#define SWEEP(s) __builtin_amdgcn_raw_buffer_load_b128(RSRC(gbase + (s) * GRAN_STRIDE, (got >> (s) & 1) ? 0u : 1024u), lane * 16, 0, 16)
#define TAKE(s, p, a, b) do { const bool ok = (__all(p[1] == tag && p[3] == tag) != 0) & ((got >> (s) & 1) == 0); \
        a = ok ? __uint_as_float(p[0]) : a; b = ok ? __uint_as_float(p[2]) : b; got |= ok ? 1u << (s) : 0u; } while (0)
#pragma unroll 1
      for (unsigned spins = 0; spins < SPIN_CAP * 8 && got != 15u; ++spins) {
        asm volatile("" ::: "memory");
        const u32x4 p0 = SWEEP(0), p1 = SWEEP(1), p2 = SWEEP(2);
        SB();
        if (!(got & 8) && all_waves(ST_DONE)) { const f32x2 s = own_sum(); a3 = s[0]; b3 = s[1]; got |= 8; }
        TAKE(0, p0, a0, b0); TAKE(1, p1, a1, b1); TAKE(2, p2, a2, b2);
      }
      if (got != 15u) a0 = b0 = __builtin_nanf("");
      const bf16x2 res = {(__bf16)fmaf(g[3], a3, fmaf(g[2], a2, fmaf(g[1], a1, g[0] * a0))),
                          (__bf16)fmaf(g[3], b3, fmaf(g[2], b2, fmaf(g[1], b1, g[0] * b0)))};
      __hip_atomic_store((gu32*)out + bh * (D / 2) + lane, __builtin_bit_cast(unsigned, res), RLX_AGENT);
    }
    return;
  }

  const char* kc = k + (size_t)(blockIdx.x / GI) * REGION;
  const char* vc = v + (size_t)(blockIdx.x / GI) * REGION;
  const int c = lane & 15;
  const unsigned voff = (wave * GI + blockIdx.x % GI) * 1024 + lane * 16;
  u32x4 qv = *reinterpret_cast<const u32x4*>(q + (size_t)bh * D * 2 + c * 16);
  SB();
  u32x4 Rc[16], Rn[16];
  {
    const auto rk = RSRC(kc, REGION);
#pragma unroll
    for (int u = 0; u < WIN; ++u) Rc[u] = LD(rk, u * LSTR);
  }
  SB();
  unsigned anchor = now32() * 16u + T0;
  __builtin_amdgcn_s_barrier();
  SB();
  PIN(qv);
  float* sc = lds + LDS_SC + wave * (NG * 64) + lane;
  const bool b8 = c & 8, b4 = c & 4, b2 = c & 2, b1 = c & 1;

#define ISSUE(j) do { if ((j) < 16) Rc[(j) & 15] = LD(rc, soc + (j) * LSTR); else Rn[(j) & 15] = LD(rn, son + ((j) - 16) * LSTR); } while (0)
#define TIGHT(lv, thr) asm volatile("s_waitcnt vmcnt(%2)\n\ts_cmp_gt_i32 %0, %1\n\ts_cbranch_scc1 1f\n\ts_waitcnt vmcnt(%3)\n1:" \
                                    :: "s"(lv), "n"(thr), "n"(WHI - 1), "n"(WHI - 2) : "scc")
#define WINDOW(u) do { PIN(Rc[u]); SB(); \
    if (LAST) { if ((u) == 0) { _Pragma("unroll") for (int j = WIN; j < 16; ++j) ISSUE(j); } } \
    else { TIGHT(level, brev4(u)); SB(); ISSUE((u) + WIN); } \
    SB(); \
    if ((u) % 8 == 0 && !LAST) { tnow = now32(); SB(); } \
    if ((u) % 8 == 2 && !LAST) { \
      int lt = (int)(tnow * 16u - anchor); \
      if (lt > cap) { anchor += lt - late; lt = late; } \
      late = lt; cap = lt + JUMP; level = lt >> GAIN_SH; anchor += TAU / 2; \
      SB(); } \
    } while (0)
#define STEPS(body) do { body(IC<0>{}); body(IC<1>{}); body(IC<2>{}); body(IC<3>{}); body(IC<4>{}); body(IC<5>{}); body(IC<6>{}); body(IC<7>{}); \
    body(IC<8>{}); body(IC<9>{}); body(IC<10>{}); body(IC<11>{}); body(IC<12>{}); body(IC<13>{}); body(IC<14>{}); body(IC<15>{}); } while (0)
  int level = 0, late = J0, cap = J0;
  unsigned tnow = 0;

  float m = -INFINITY;
#pragma unroll 1
  for (int g = 0; g < NG; ++g) {
    constexpr bool LAST = false;
    const auto rc = RSRC(kc, REGION);
    const auto rn = RSRC(g + 1 < NG ? kc : vc, REGION);
    const unsigned soc = g * GBYTES, son = g + 1 < NG ? soc + GBYTES : 0u;
    float x[16];
    auto body = [&](auto uc) {
      constexpr int u = decltype(uc)::value;
      WINDOW(u);
      const u32x4 kk = Rc[u];
      float d = dot2(kk[0], qv[0], 0.f);
      d = dot2(kk[1], qv[1], d);
      d = dot2(kk[2], qv[2], d);
      x[u] = dot2(kk[3], qv[3], d);
      if constexpr (u & 1) {
        x[u] = tmerge<XOR8>(x[u - 1], x[u], b8);
        if constexpr (u & 2) {
          x[u] = tmerge<XOR7>(x[u - 2], x[u], b4);
          if constexpr (u & 4) {
            x[u] = tmerge<XOR2>(x[u - 4], x[u], b2);
            if constexpr (u & 8) x[u] = tmerge<XOR1>(x[u - 8], x[u], b1);
          }
        }
      }
      PIN(x[u]);
    };
    STEPS(body);
    sc[g * 64] = x[15];
    m = fmaxf(m, x[15]);
#pragma unroll
    for (int u = 0; u < WIN; ++u) Rc[u] = Rn[u];
  }
  float snext = sc[0];
  m = wave_max(m);
  lds[LDS_MAX + wave] = m;
  LDS_DRAIN();
  st[wave] = ST_KDONE;
  m = finite_ref(m);

  f32x2 acc[4];
#pragma unroll
  for (int i = 0; i < 4; ++i) acc[i] = f32x2{0.f, 0.f};
  auto vgroup = [&](int g, auto last) {
    constexpr bool LAST = decltype(last)::value;
    const auto rc = RSRC(vc, REGION);
    const auto rn = rc;
    const unsigned soc = g * GBYTES, son = soc + GBYTES;
    float p;
    auto body = [&](auto uc) {
      constexpr int u = decltype(uc)::value;
      WINDOW(u);
      if constexpr (u == 0) {
        p = ex2((snext - m) * C2);
        if (!LAST) snext = sc[(g + 1) * 64];
      }
      const u32x4 vv = Rc[u];
      const float pb = dpp<BCAST + brev4(u)>(p);
      const f32x2 pp = {pb, pb};
#pragma unroll
      for (int i = 0; i < 4; ++i) {
        const f32x2 vf = {__builtin_bit_cast(float, vv[i] << 16), __builtin_bit_cast(float, vv[i] & 0xffff0000u)};
        acc[i] = __builtin_elementwise_fma(pp, vf, acc[i]);
      }
      PIN(acc[0]); PIN(acc[1]); PIN(acc[2]); PIN(acc[3]);
    };
    STEPS(body);
    if (!LAST) {
#pragma unroll
      for (int u = 0; u < WIN; ++u) Rc[u] = Rn[u];
    }
  };
#pragma unroll 1
  for (int g = 0; g < NG - 1; ++g) vgroup(g, IC<0>{});
  st[wave] = ST_LAST;
  vgroup(NG - 1, IC<1>{});
  float* dst = lds + LDS_ACC + wave * (4 * D) + lane * 8;
  *reinterpret_cast<f32x4*>(dst) = f32x4{acc[0][0], acc[0][1], acc[1][0], acc[1][1]};
  *reinterpret_cast<f32x4*>(dst + 4) = f32x4{acc[2][0], acc[2][1], acc[3][0], acc[3][1]};
  LDS_DRAIN();
  st[wave] = ST_DONE;
}

extern "C" void kernel_launch(void* const* d_in, const int* in_sizes, int n_in, void* d_out, int out_size, void* d_ws,
                              size_t ws_size, hipStream_t stream) {
  if (n_in != 3 || in_sizes[0] != BH * D || in_sizes[1] != BH * SKV * D || in_sizes[2] != BH * SKV * D || out_size != BH * D ||
      ws_size < WS_BYTES)
    return;
  (void)hipFuncSetAttribute((const void*)attn_decode, hipFuncAttributeMaxDynamicSharedMemorySize, LDS_BYTES);
  attn_decode<<<BH * NSPLIT, (WAVES + 1) * 64, LDS_BYTES, stream>>>((const char*)d_in[0], (const char*)d_in[1], (const char*)d_in[2],
                                                                     (char*)d_ws, (__hip_bfloat16*)d_out);
}
```

```cpp
#include <hip/hip_runtime.h>
#include <hip/hip_bf16.h>
#include <cstdint>
#include <type_traits>

constexpr int BH = 64, SKV = 16384, D = 128, NSPLIT = 4, WAVES = 4;
constexpr unsigned HEAD = SKV * D * 2;
constexpr int NG = SKV / NSPLIT / (WAVES * 64);
constexpr unsigned MAP[14] = {0x2000, 0x2,   0xc0, 0x10,   0x1, 0x8,   0x20, 0x40, 0x2408, 0x100,
                                0x240, 0x800, 0x44, 0x1084};
constexpr unsigned sel2(int i, unsigned x) { return ((x & 1) ? MAP[i] : 0u) ^ ((x & 2) ? MAP[i + 1] : 0u); }
constexpr unsigned sel4(int i, unsigned x) { return sel2(i, x) ^ sel2(i + 2, x >> 2); }
constexpr bool map_invertible() {
  unsigned r[14] = {};
  for (int i = 0; i < 14; ++i) r[i] = MAP[i];
  for (int b = 0; b < 14; ++b) {
    int p = -1;
    for (int i = b; i < 14; ++i) if ((r[i] >> b) & 1) { p = i; break; }
    if (p < 0) return false;
    const unsigned t = r[p]; r[p] = r[b]; r[b] = t;
    for (int i = 0; i < 14; ++i) if (i != b && ((r[i] >> b) & 1)) r[i] ^= r[b];
  }
  for (int i = 0; i < 14; ++i) if (r[i] != 1u << i) return false;
  return true;
}
static_assert(map_invertible(), "every key exactly once");
constexpr int WIN = 8;
constexpr int WHI = 6;
constexpr int T0 = 100 * 16;
constexpr int TAU = 3405;
constexpr int GAIN_SH = 6, LMIN = 2;
constexpr int JUMP = 120 * 16, J0 = 30 * 16;
constexpr int LDS_BYTES = 90112;
template <int N> using IC = std::integral_constant<int, N>;

using bf16x2 = __attribute__((ext_vector_type(2))) __bf16;
using u32x4 = __attribute__((ext_vector_type(4))) unsigned;
using f32x4 = __attribute__((ext_vector_type(4))) float;
using f32x2 = __attribute__((ext_vector_type(2))) float;
typedef __attribute__((address_space(1))) unsigned long long gu64;
typedef __attribute__((address_space(1))) unsigned gu32;
typedef volatile __attribute__((address_space(3))) unsigned lu32;
#define RLX_AGENT __ATOMIC_RELAXED, __HIP_MEMORY_SCOPE_AGENT

__device__ __forceinline__ float dot2(unsigned k, unsigned q, float acc) {
  return __builtin_amdgcn_fdot2_f32_bf16(__builtin_bit_cast(bf16x2, k), __builtin_bit_cast(bf16x2, q), acc, false);
}
template <int CTRL> __device__ __forceinline__ float dpp(float x) {
  return __builtin_bit_cast(float, __builtin_amdgcn_mov_dpp(__builtin_bit_cast(int, x), CTRL, 0xf, 0xf, true));
}
constexpr int XOR1 = 0xB1, XOR2 = 0x4E, XOR7 = 0x141, XOR8 = 0x128, ROR4 = 0x124, ROR2 = 0x122, ROR1 = 0x121, BCAST = 0x150;
template <int CTRL> __device__ __forceinline__ float tmerge(float a, float b, bool hi) {
  const float ta = a + dpp<CTRL>(a), tb = b + dpp<CTRL>(b);
  return hi ? tb : ta;
}
__device__ __forceinline__ float wave_max(float x) {
  x = fmaxf(x, dpp<XOR8>(x)); x = fmaxf(x, dpp<ROR4>(x)); x = fmaxf(x, dpp<ROR2>(x)); x = fmaxf(x, dpp<ROR1>(x));
  auto s = __builtin_amdgcn_permlane16_swap(__float_as_uint(x), __float_as_uint(x), false, false);
  x = fmaxf(__uint_as_float(s[0]), __uint_as_float(s[1]));
  auto t = __builtin_amdgcn_permlane32_swap(__float_as_uint(x), __float_as_uint(x), false, false);
  return fmaxf(__uint_as_float(t[0]), __uint_as_float(t[1]));
}
__device__ __forceinline__ float wave_sum(float x) {
  x += dpp<XOR8>(x); x += dpp<ROR4>(x); x += dpp<ROR2>(x); x += dpp<ROR1>(x);
  auto s = __builtin_amdgcn_permlane16_swap(__float_as_uint(x), __float_as_uint(x), false, false);
  x = __uint_as_float(s[0]) + __uint_as_float(s[1]);
  auto t = __builtin_amdgcn_permlane32_swap(__float_as_uint(x), __float_as_uint(x), false, false);
  return __uint_as_float(t[0]) + __uint_as_float(t[1]);
}
constexpr int brev4(int u) { return ((u & 1) << 3) | ((u & 2) << 1) | ((u & 4) >> 1) | ((u & 8) >> 3); }
__device__ __forceinline__ unsigned now32() { return (unsigned)__builtin_amdgcn_s_memrealtime(); }
__device__ __forceinline__ float ex2(float x) { return __builtin_amdgcn_exp2f(x); }
__device__ __forceinline__ float finite_ref(float m) { return m == -INFINITY ? 0.f : m; }

constexpr float C2 = 0.088388347648318440f * 1.4426950408889634f;
constexpr int LDS_SC = 0, LDS_ACC = WAVES * NG * 64, LDS_MAX = LDS_ACC + WAVES * 4 * D, LDS_ST = LDS_MAX + 4;
constexpr unsigned ST_KDONE = 1, ST_LAST = 2, ST_DONE = 3;
constexpr size_t WS_EPOCH = 0, WS_GRAN = 65536, GRAN_STRIDE = 2048, WS_BYTES = WS_GRAN + (size_t)BH * NSPLIT * GRAN_STRIDE;
constexpr unsigned SPIN_CAP = 1u << 18;

#define RSRC(p, n) __builtin_amdgcn_make_buffer_rsrc((void*)(p), 0, (n), 0x00020000)
#define LD(rs, vg, u) __builtin_amdgcn_raw_buffer_load_b128(rs, (vg) ^ (sel4(6, u) << 8), 0, 2)
#define PIN(x) asm volatile("" : "+v"(x))
#define SB() __builtin_amdgcn_sched_barrier(0)
#define LDS_DRAIN() asm volatile("s_waitcnt lgkmcnt(0)" ::: "memory")

__global__ void __launch_bounds__((WAVES + 1) * 64, 2)
attn_decode(const char* __restrict__ q, const char* __restrict__ k, const char* __restrict__ v, char* __restrict__ ws,
            __hip_bfloat16* __restrict__ out) {
  extern __shared__ __attribute__((aligned(16))) float lds[];
  const int bh = blockIdx.x / NSPLIT, split = blockIdx.x % NSPLIT;
  const int tid = threadIdx.x, lane = tid & 63, wave = __builtin_amdgcn_readfirstlane(tid >> 6);
  lu32* st = (lu32*)(lds + LDS_ST);

  asm volatile("s_cmp_lg_u32 %0, 4\n\ts_cbranch_scc1 1f\n\tds_write_b32 %1, %2\n\ts_waitcnt lgkmcnt(0)\n\ts_barrier\n1:"
               :: "s"(wave), "v"((unsigned)(uintptr_t)(st + (lane & 3))), "v"(0u) : "scc", "memory");
  const int c = lane & 15;
  unsigned voff = ((sel2(0, lane >> 4) ^ sel2(2, wave) ^ sel2(4, split)) << 8) | (c * 16), qoff = bh * D * 2 + c * 16;
  PIN(voff); PIN(qoff);
  asm volatile("" :: "s"(q), "s"(k), "s"(v));

  if (wave == WAVES) {
    gu32* ep = (gu32*)(ws + WS_EPOCH + (size_t)bh * 128);
    const unsigned epoch = __hip_atomic_load(ep, RLX_AGENT);
    const unsigned tag = epoch * 0x9E3779B1u + 0x7F4A7C15u;
    const char* gbase = ws + WS_GRAN + (size_t)bh * NSPLIT * GRAN_STRIDE;
    auto all_waves = [&](unsigned s) { return __all(st[lane & 3] >= s); };
    auto any_wave = [&](unsigned s) { return __any(st[lane & 3] >= s); };
    while (!all_waves(ST_KDONE)) __builtin_amdgcn_s_sleep(32);
    asm volatile("" ::: "memory");
    const f32x4 mw = *reinterpret_cast<const f32x4*>(lds + LDS_MAX);
    const float M = fmaxf(fmaxf(mw[0], mw[1]), fmaxf(mw[2], mw[3])), Ms = finite_ref(M);
    float L = 0.f;
#pragma unroll 4
    for (int i = 0; i < WAVES * NG / 4; ++i) {
      const f32x4 s = *reinterpret_cast<const f32x4*>(lds + LDS_SC + i * 256 + lane * 4);
      L += (ex2((s[0] - Ms) * C2) + ex2((s[1] - Ms) * C2)) + (ex2((s[2] - Ms) * C2) + ex2((s[3] - Ms) * C2));
    }
    L = wave_sum(L);
    float f[WAVES];
#pragma unroll
    for (int w = 0; w < WAVES; ++w) f[w] = ex2((mw[w] - Ms) * C2);
    auto own_sum = [&]() {
      asm volatile("" ::: "memory");
      f32x2 t[WAVES * 4];
#pragma unroll
      for (int i = 0; i < WAVES * 4; ++i) t[i] = *reinterpret_cast<const f32x2*>(lds + LDS_ACC + i * D + 2 * lane);
      SB();
#pragma unroll
      for (int w = 0; w < WAVES; ++w) t[4 * w] = ((t[4 * w] + t[4 * w + 1]) + (t[4 * w + 2] + t[4 * w + 3])) * f[w];
      return (t[0] + t[4]) + (t[8] + t[12]);
    };
    if (split != NSPLIT - 1) {
      if (lane < 16)
        __hip_atomic_store((gu64*)(gbase + split * GRAN_STRIDE) + D + lane, ((unsigned long long)tag << 32) | __float_as_uint(lane & 1 ? L : M), RLX_AGENT);
#pragma unroll
      for (int w = 0; w < WAVES; ++w) PIN(f[w]);
      while (!any_wave(ST_LAST)) __builtin_amdgcn_s_sleep(16);
      while (!all_waves(ST_DONE)) {}
      const f32x2 s = own_sum();
      __builtin_amdgcn_raw_buffer_store_b128(u32x4{__float_as_uint(s[0]), tag, __float_as_uint(s[1]), tag},
                                             RSRC(gbase + split * GRAN_STRIDE, 1024), lane * 16, 0, 16);
    } else {
      __builtin_amdgcn_s_sleep(127); __builtin_amdgcn_s_sleep(127);
      unsigned long long x;
#pragma unroll 1
      for (unsigned spins = 0; spins < SPIN_CAP; ++spins) {
        x = __hip_atomic_load((gu64*)gbase + (lane < 6 ? (lane >> 1) * (GRAN_STRIDE / 8) + D + (lane & 1) : D), RLX_AGENT);
        if (__all((unsigned)(x >> 32) == tag)) break;
        __builtin_amdgcn_s_sleep(64);
      }
      if (lane < 32) __hip_atomic_store(ep + lane, epoch + 1u, RLX_AGENT);
      float ms[NSPLIT], ls[NSPLIT], g[NSPLIT];
#pragma unroll
      for (int s = 0; s < NSPLIT - 1; ++s) {
        ms[s] = __uint_as_float(__builtin_amdgcn_readlane((unsigned)x, 2 * s));
        ls[s] = __uint_as_float(__builtin_amdgcn_readlane((unsigned)x, 2 * s + 1));
      }
      ms[NSPLIT - 1] = M; ls[NSPLIT - 1] = L;
      const float MM = fmaxf(fmaxf(ms[0], ms[1]), fmaxf(ms[2], ms[3]));
      float LL = 0.f;
#pragma unroll
      for (int s = 0; s < NSPLIT; ++s) { g[s] = ex2((ms[s] - MM) * C2); LL = fmaf(g[s], ls[s], LL); }
      const float inv = __all((unsigned)(x >> 32) == tag) ? 1.f / LL : __builtin_nanf("");
#pragma unroll
      for (int s = 0; s < NSPLIT; ++s) { g[s] *= inv; PIN(g[s]); PIN(f[s]); }
      while (!any_wave(ST_LAST)) __builtin_amdgcn_s_sleep(16);
      float a0 = 0.f, a1 = 0.f, a2 = 0.f, a3 = 0.f, b0 = 0.f, b1 = 0.f, b2 = 0.f, b3 = 0.f;
      unsigned got = 0;
#define SWEEP(s) __builtin_amdgcn_raw_buffer_load_b128(RSRC(gbase + (s) * GRAN_STRIDE, (got >> (s) & 1) ? 0u : 1024u), lane * 16, 0, 16)
#define TAKE(s, p, a, b) do { const bool ok = (__all(p[1] == tag && p[3] == tag) != 0) & ((got >> (s) & 1) == 0); \
        a = ok ? __uint_as_float(p[0]) : a; b = ok ? __uint_as_float(p[2]) : b; got |= ok ? 1u << (s) : 0u; } while (0)
#pragma unroll 1
      for (unsigned spins = 0; spins < SPIN_CAP * 8 && got != 15u; ++spins) {
        asm volatile("" ::: "memory");
        const u32x4 p0 = SWEEP(0), p1 = SWEEP(1), p2 = SWEEP(2);
        SB();
        if (!(got & 8) && all_waves(ST_DONE)) { const f32x2 s = own_sum(); a3 = s[0]; b3 = s[1]; got |= 8; }
        TAKE(0, p0, a0, b0); TAKE(1, p1, a1, b1); TAKE(2, p2, a2, b2);
      }
      if (got != 15u) a0 = b0 = __builtin_nanf("");
      const bf16x2 res = {(__bf16)fmaf(g[3], a3, fmaf(g[2], a2, fmaf(g[1], a1, g[0] * a0))),
                          (__bf16)fmaf(g[3], b3, fmaf(g[2], b2, fmaf(g[1], b1, g[0] * b0)))};
      __hip_atomic_store((gu32*)out + bh * (D / 2) + lane, __builtin_bit_cast(unsigned, res), RLX_AGENT);
    }
    return;
  }

  const char* kc = k + (size_t)bh * HEAD;
  const char* vc = v + (size_t)bh * HEAD;
  u32x4 qv = *reinterpret_cast<const u32x4*>(q + qoff);
  SB();
  u32x4 Rc[16], Rn[16];
  {
    const auto rk = RSRC(kc, HEAD);
#pragma unroll
    for (int u = 0; u < WIN; ++u) Rc[u] = LD(rk, voff, u);
  }
  SB();
  unsigned anchor = now32() * 16u + T0;
  __builtin_amdgcn_s_barrier();
  SB();
  PIN(qv);
  float* sc = lds + LDS_SC + wave * (NG * 64) + lane;
  const bool b8 = c & 8, b4 = c & 4, b2 = c & 2, b1 = c & 1;

#define ISSUE(j) do { if ((j) < 16) Rc[(j) & 15] = LD(rc, oc, (j) & 15); else Rn[(j) & 15] = LD(rn, on, (j) & 15); } while (0)
#define TIGHT(lv, thr) asm volatile("s_waitcnt vmcnt(%2)\n\ts_cmp_gt_i32 %0, %1\n\ts_cbranch_scc1 1f\n\ts_waitcnt vmcnt(%3)\n1:" \
                                    :: "s"(lv), "n"(thr), "n"(WHI - 1), "n"(WHI - 2) : "scc")
#define WINDOW(u) do { PIN(Rc[u]); SB(); \
    if (LAST) { if ((u) == 0) { _Pragma("unroll") for (int j = WIN; j < 16; ++j) ISSUE(j); } } \
    else { TIGHT(level, brev4(u)); SB(); ISSUE((u) + WIN); } \
    SB(); \
    if ((u) % 8 == 0 && !LAST) { tnow = now32(); SB(); } \
    if ((u) % 8 == 2 && !LAST) { \
      int lt = (int)(tnow * 16u - anchor); \
      if (lt > cap) { anchor += lt - late; lt = late; } \
      late = early ? J0 : lt; cap = early ? J0 : lt + JUMP; level = max(lt >> GAIN_SH, 0) + LMIN; anchor += TAU / 2; \
      SB(); } \
    } while (0)
#define STEPS(body) do { body(IC<0>{}); body(IC<1>{}); body(IC<2>{}); body(IC<3>{}); body(IC<4>{}); body(IC<5>{}); body(IC<6>{}); body(IC<7>{}); \
    body(IC<8>{}); body(IC<9>{}); body(IC<10>{}); body(IC<11>{}); body(IC<12>{}); body(IC<13>{}); body(IC<14>{}); body(IC<15>{}); } while (0)
  int level = LMIN, late = J0, cap = J0;
  unsigned tnow = 0;

  float m = -INFINITY;
#pragma unroll 1
  for (int g = 0; g < NG; ++g) {
    constexpr bool LAST = false;
    const bool early = g < 2;
    const auto rc = RSRC(kc, HEAD);
    const auto rn = RSRC(g + 1 < NG ? kc : vc, HEAD);
    const unsigned oc = voff ^ (sel4(10, g) << 8), on = voff ^ (sel4(10, g + 1) << 8);
    float x[16];
    auto body = [&](auto uc) {
      constexpr int u = decltype(uc)::value;
      WINDOW(u);
      const u32x4 kk = Rc[u];
      float d = dot2(kk[0], qv[0], 0.f);
      d = dot2(kk[1], qv[1], d);
      d = dot2(kk[2], qv[2], d);
      x[u] = dot2(kk[3], qv[3], d);
      if constexpr (u & 1) {
        x[u] = tmerge<XOR8>(x[u - 1], x[u], b8);
        if constexpr (u & 2) {
          x[u] = tmerge<XOR7>(x[u - 2], x[u], b4);
          if constexpr (u & 4) {
            x[u] = tmerge<XOR2>(x[u - 4], x[u], b2);
            if constexpr (u & 8) x[u] = tmerge<XOR1>(x[u - 8], x[u], b1);
          }
        }
      }
      PIN(x[u]);
    };
    STEPS(body);
    sc[g * 64] = x[15];
    m = fmaxf(m, x[15]);
#pragma unroll
    for (int u = 0; u < WIN; ++u) Rc[u] = Rn[u];
  }
  float snext = sc[0];
  m = wave_max(m);
  lds[LDS_MAX + wave] = m;
  LDS_DRAIN();
  st[wave] = ST_KDONE;
  m = finite_ref(m);

  f32x2 acc[4];
#pragma unroll
  for (int i = 0; i < 4; ++i) acc[i] = f32x2{0.f, 0.f};
  auto vgroup = [&](int g, auto last) {
    constexpr bool LAST = decltype(last)::value, early = false;
    const auto rc = RSRC(vc, HEAD);
    const auto rn = rc;
    const unsigned oc = voff ^ (sel4(10, g) << 8), on = voff ^ (sel4(10, g + 1) << 8);
    float p;
    auto body = [&](auto uc) {
      constexpr int u = decltype(uc)::value;
      WINDOW(u);
      if constexpr (u == 0) {
        p = ex2((snext - m) * C2);
        if (!LAST) snext = sc[(g + 1) * 64];
      }
      const u32x4 vv = Rc[u];
      const float pb = dpp<BCAST + brev4(u)>(p);
      const f32x2 pp = {pb, pb};
#pragma unroll
      for (int i = 0; i < 4; ++i) {
        const f32x2 vf = {__builtin_bit_cast(float, vv[i] << 16), __builtin_bit_cast(float, vv[i] & 0xffff0000u)};
        acc[i] = __builtin_elementwise_fma(pp, vf, acc[i]);
      }
      PIN(acc[0]); PIN(acc[1]); PIN(acc[2]); PIN(acc[3]);
    };
    STEPS(body);
    if (!LAST) {
#pragma unroll
      for (int u = 0; u < WIN; ++u) Rc[u] = Rn[u];
    }
  };
#pragma unroll 1
  for (int g = 0; g < NG - 1; ++g) vgroup(g, IC<0>{});
  st[wave] = ST_LAST;
  vgroup(NG - 1, IC<1>{});
  float* dst = lds + LDS_ACC + wave * (4 * D) + lane * 8;
  *reinterpret_cast<f32x4*>(dst) = f32x4{acc[0][0], acc[0][1], acc[1][0], acc[1][1]};
  *reinterpret_cast<f32x4*>(dst + 4) = f32x4{acc[2][0], acc[2][1], acc[3][0], acc[3][1]};
  LDS_DRAIN();
  st[wave] = ST_DONE;
}

extern "C" void kernel_launch(void* const* d_in, const int* in_sizes, int n_in, void* d_out, int out_size, void* d_ws,
                              size_t ws_size, hipStream_t stream) {
  if (n_in != 3 || in_sizes[0] != BH * D || in_sizes[1] != BH * SKV * D || in_sizes[2] != BH * SKV * D || out_size != BH * D ||
      ws_size < WS_BYTES)
    return;
  (void)hipFuncSetAttribute((const void*)attn_decode, hipFuncAttributeMaxDynamicSharedMemorySize, LDS_BYTES);
  attn_decode<<<BH * NSPLIT, (WAVES + 1) * 64, LDS_BYTES, stream>>>((const char*)d_in[0], (const char*)d_in[1], (const char*)d_in[2],
                                                                     (char*)d_ws, (__hip_bfloat16*)d_out);
}
```
